```python
import torch

def _softmax(x, axis=-1):
    return torch.softmax(x, dim=axis)
def _gelu(x):
    return torch.nn.functional.gelu(x, approximate="tanh")
def _silu(x):
    return torch.nn.functional.silu(x)
def _sigmoid(x):
    return torch.sigmoid(x)
def _layernorm(x, axis=-1, eps=1e-5):
    m = x.mean(dim=axis, keepdim=True)
    v = x.var(dim=axis, keepdim=True, unbiased=False)
    return (x - m) / torch.sqrt(v + eps)
def _scatter_add(tgt, idx, src, axis=0):
    return tgt.clone().index_add_(axis, idx, src)
def _segment_sum(src, seg, n):
    out = torch.zeros((n,) + src.shape[1:], dtype=src.dtype)
    return out.index_add_(0, seg, src)
def _pad(x, pw):
    flat = tuple(v for pair in reversed(pw) for v in pair)
    return torch.nn.functional.pad(x, flat)

kernel_name = 'standalone__rmsnorm__sampling'

def setup_inputs():

    _w = (lambda a: (lambda t: t.to(torch.bfloat16) if t.is_floating_point() else t)(torch.as_tensor(a)))
    B, S, D = 1024, 16, 576
    return {
        "x": _w((lambda *s: torch.randn(*s, dtype=torch.bfloat16) * 0.05)(B, S, D)),
        "g": _w((lambda *s: torch.randn(*s, dtype=torch.bfloat16) * 0.05)(D) + 1.0),
    }

def reference(x, g):
    rms = torch.sqrt((x * x).mean(axis=-1, keepdims=True) + 1e-6)
    return x / rms * g
```

```cpp
#include <hip/hip_runtime.h>
#include <stdint.h>

typedef unsigned int u32x4 __attribute__((ext_vector_type(4)));
typedef float f32x2 __attribute__((ext_vector_type(2)));
typedef __bf16 bf16x2 __attribute__((ext_vector_type(2)));

static constexpr int D = 576;
static constexpr int CH = D / 8;

__device__ __forceinline__ float lo_f(unsigned u) { return __uint_as_float(u << 16); }
__device__ __forceinline__ float hi_f(unsigned u) { return __uint_as_float(u & 0xffff0000u); }
__device__ __forceinline__ unsigned pack_bf16(float a, float b) {
  bf16x2 r; r.x = (__bf16)a; r.y = (__bf16)b;
  return __builtin_bit_cast(unsigned, r);
}

template <int CTRL>
__device__ __forceinline__ float dpp_add(float v) {
  int t = __builtin_amdgcn_update_dpp(0, __float_as_int(v), CTRL, 0xf, 0xf, true);
  return v + __int_as_float(t);
}

__global__ __launch_bounds__(256) void rmsnorm_k(const u32x4* __restrict__ x, const u32x4* __restrict__ g,
                                                 u32x4* __restrict__ y, int rows) {
  int tid = blockIdx.x * 256 + threadIdx.x;
  int row = tid >> 3;
  int j = tid & 7;
  if (row >= rows) return;
  const u32x4* xr = x + (size_t)row * CH + j;
  u32x4 v[9], gv[9];
#pragma unroll
  for (int k = 0; k < 9; ++k) v[k] = xr[k * 8];
#pragma unroll
  for (int k = 0; k < 9; ++k) gv[k] = g[j + k * 8];
  float s0 = 0.f, s1 = 0.f;
#pragma unroll
  for (int k = 0; k < 9; ++k) {
#pragma unroll
    for (int e = 0; e < 4; ++e) {
      float a = lo_f(v[k][e]), b = hi_f(v[k][e]);
      s0 = fmaf(a, a, s0);
      s1 = fmaf(b, b, s1);
    }
  }
  float ss = s0 + s1;
  ss = dpp_add<0xB1>(ss);
  ss = dpp_add<0x4E>(ss);
  ss = dpp_add<0x141>(ss);
  float inv = rsqrtf(ss * (1.0f / D) + 1e-6f);
  u32x4* yr = y + (size_t)row * CH + j;
#pragma unroll
  for (int k = 0; k < 9; ++k) {
    u32x4 o;
#pragma unroll
    for (int e = 0; e < 4; ++e) {
      float a = lo_f(v[k][e]) * inv * lo_f(gv[k][e]);
      float b = hi_f(v[k][e]) * inv * hi_f(gv[k][e]);
      o[e] = pack_bf16(a, b);
    }
    yr[k * 8] = o;
  }
}

extern "C" void kernel_launch(void* const* d_in, const int* in_sizes, int n_in, void* d_out, int out_size,
                              void* d_ws, size_t ws_size, hipStream_t stream) {
  const u32x4* x = (const u32x4*)d_in[0];
  const u32x4* g = (const u32x4*)d_in[1];
  u32x4* y = (u32x4*)d_out;
  int rows = in_sizes[0] / D;
  int blocks = (rows * 8 + 255) / 256;
  rmsnorm_k<<<blocks, 256, 0, stream>>>(x, g, y, rows);
}
```

```cpp
#include <hip/hip_runtime.h>
#include <stdint.h>

typedef unsigned int u32x4 __attribute__((ext_vector_type(4)));
typedef float f32x2 __attribute__((ext_vector_type(2)));
typedef __bf16 bf16x2 __attribute__((ext_vector_type(2)));

static constexpr int D = 576;
static constexpr int CH = D / 8;

__device__ __forceinline__ float lo_f(unsigned u) { return __uint_as_float(u << 16); }
__device__ __forceinline__ float hi_f(unsigned u) { return __uint_as_float(u & 0xffff0000u); }
__device__ __forceinline__ unsigned pack_bf16(float a, float b) {
  bf16x2 r; r.x = (__bf16)a; r.y = (__bf16)b;
  return __builtin_bit_cast(unsigned, r);
}

template <int CTRL>
__device__ __forceinline__ float dpp_add(float v) {
  int t = __builtin_amdgcn_update_dpp(0, __float_as_int(v), CTRL, 0xf, 0xf, true);
  return v + __int_as_float(t);
}

__global__ __launch_bounds__(256) void rmsnorm_k(const u32x4* __restrict__ x, const u32x4* __restrict__ g,
                                                 u32x4* __restrict__ y, int rows) {
  int tid = blockIdx.x * 256 + threadIdx.x;
  int row = tid >> 3;
  int j = tid & 7;
  if (row >= rows) return;
  const u32x4* xr = x + (size_t)row * CH + j;
  u32x4 v[9], gv[9];
#pragma unroll
  for (int k = 0; k < 9; ++k) v[k] = xr[k * 8];
#pragma unroll
  for (int k = 0; k < 9; ++k) gv[k] = g[j + k * 8];
  float s0 = 0.f, s1 = 0.f;
#pragma unroll
  for (int k = 0; k < 9; ++k) {
#pragma unroll
    for (int e = 0; e < 4; ++e) {
      float a = lo_f(v[k][e]), b = hi_f(v[k][e]);
      s0 = fmaf(a, a, s0);
      s1 = fmaf(b, b, s1);
    }
  }
  float ss = s0 + s1;
  ss = dpp_add<0xB1>(ss);
  ss = dpp_add<0x4E>(ss);
  ss = dpp_add<0x141>(ss);
  float inv = rsqrtf(ss * (1.0f / D) + 1e-6f);
  u32x4* yr = y + (size_t)row * CH + j;
#pragma unroll
  for (int k = 0; k < 9; ++k) {
    u32x4 o;
#pragma unroll
    for (int e = 0; e < 4; ++e) {
      float a = lo_f(v[k][e]) * inv * lo_f(gv[k][e]);
      float b = hi_f(v[k][e]) * inv * hi_f(gv[k][e]);
      o[e] = pack_bf16(a, b);
    }
    yr[k * 8] = o;
  }
}


#include <cstdio>
#include <vector>
#include <algorithm>
__global__ void empty_k() {}
__global__ void fill_k(uint4* p, size_t n, unsigned seed) {
  size_t i = (size_t)blockIdx.x * blockDim.x + threadIdx.x;
  size_t stride = (size_t)gridDim.x * blockDim.x;
  for (; i < n; i += stride) { unsigned h = (unsigned)i * 2654435761u + seed; p[i] = make_uint4(h, h ^ 0x9e3779b9u, h * 31u, h + 7u); }
}
extern "C" void kernel_launch(void* const* d_in, const int* in_sizes, int n_in, void* d_out, int out_size,
                              void* d_ws, size_t ws_size, hipStream_t stream) {
  const u32x4* x = (const u32x4*)d_in[0];
  const u32x4* g = (const u32x4*)d_in[1];
  u32x4* y = (u32x4*)d_out;
  int rows = in_sizes[0] / D;
  int blocks = (rows * 8 + 255) / 256;
  hipStreamCaptureStatus cs; hipStreamIsCapturing(stream, &cs);
  fprintf(stderr, "DIAG call: capturing=%d x=%p g=%p out=%p ws=%p ws_size=%zu stream=%p\n", (int)cs, d_in[0], d_in[1], d_out, d_ws, ws_size, (void*)stream);
  if (cs == hipStreamCaptureStatusNone) {
    void* ps[4] = {d_in[0], d_in[1], d_out, d_ws};
    for (int i = 0; i < 4; ++i) {
      hipPointerAttribute_t a; hipError_t e = hipPointerGetAttributes(&a, ps[i]);
      hipDeviceptr_t base = 0; size_t sz = 0; hipError_t e2 = hipMemGetAddressRange(&base, &sz, ps[i]);
      fprintf(stderr, "DIAG ptr %d: err=%d type=%d dev=%d managed=%d flags=%u | range err=%d base=%p size=%zu\n", i, (int)e, (int)a.type, a.device, a.isManaged, a.allocationFlags, (int)e2, (void*)base, sz);
    }
    hipEvent_t e0, e1; hipEventCreate(&e0); hipEventCreate(&e1);
    for (int mode = 0; mode < 3; ++mode) {
      std::vector<float> t;
      for (int r = 0; r < 15; ++r) {
        fill_k<<<2048, 256, 0, stream>>>((uint4*)d_ws, ws_size / 16, r);
        hipEventRecord(e0, stream);
        if (mode == 0) empty_k<<<1, 64, 0, stream>>>();
        else if (mode == 1) rmsnorm_k<<<blocks, 256, 0, stream>>>(x, g, y, rows);
        else for (int k = 0; k < 20; ++k) rmsnorm_k<<<blocks, 256, 0, stream>>>(x, g, y, rows);
        hipEventRecord(e1, stream); hipStreamSynchronize(stream);
        float ms; hipEventElapsedTime(&ms, e0, e1); t.push_back(ms * 1000.f / (mode == 2 ? 20 : 1));
      }
      std::sort(t.begin(), t.end());
      fprintf(stderr, "DIAG mode %d: med %.2f min %.2f\n", mode, t[t.size() / 2], t[0]);
    }
  }
  rmsnorm_k<<<blocks, 256, 0, stream>>>(x, g, y, rows);
}
```

```cpp
#include <hip/hip_runtime.h>
#include <stdint.h>

typedef unsigned int u32x4 __attribute__((ext_vector_type(4)));
typedef float f32x2 __attribute__((ext_vector_type(2)));
typedef __bf16 bf16x2 __attribute__((ext_vector_type(2)));

static constexpr int D = 576;
static constexpr int CH = D / 8;

__device__ __forceinline__ float lo_f(unsigned u) { return __uint_as_float(u << 16); }
__device__ __forceinline__ float hi_f(unsigned u) { return __uint_as_float(u & 0xffff0000u); }
__device__ __forceinline__ unsigned pack_bf16(float a, float b) {
  bf16x2 r; r.x = (__bf16)a; r.y = (__bf16)b;
  return __builtin_bit_cast(unsigned, r);
}

template <int CTRL>
__device__ __forceinline__ float dpp_add(float v) {
  int t = __builtin_amdgcn_update_dpp(0, __float_as_int(v), CTRL, 0xf, 0xf, true);
  return v + __int_as_float(t);
}

__global__ __launch_bounds__(256) void rmsnorm_k(const u32x4* __restrict__ x, const u32x4* __restrict__ g,
                                                 u32x4* __restrict__ y, int rows) {
  unsigned long long t0 = __builtin_amdgcn_s_memrealtime();
  int tid = blockIdx.x * 256 + threadIdx.x;
  int row = tid >> 3;
  int j = tid & 7;
  if (row >= rows) return;
  const u32x4* xr = x + (size_t)row * CH + j;
  u32x4 v[9], gv[9];
#pragma unroll
  for (int k = 0; k < 9; ++k) v[k] = xr[k * 8];
#pragma unroll
  for (int k = 0; k < 9; ++k) gv[k] = g[j + k * 8];
  float s0 = 0.f, s1 = 0.f;
#pragma unroll
  for (int k = 0; k < 9; ++k) {
#pragma unroll
    for (int e = 0; e < 4; ++e) {
      float a = lo_f(v[k][e]), b = hi_f(v[k][e]);
      s0 = fmaf(a, a, s0);
      s1 = fmaf(b, b, s1);
    }
  }
  float ss = s0 + s1;
  ss = dpp_add<0xB1>(ss);
  ss = dpp_add<0x4E>(ss);
  ss = dpp_add<0x141>(ss);
  float inv = rsqrtf(ss * (1.0f / D) + 1e-6f);
  u32x4* yr = y + (size_t)row * CH + j;
#pragma unroll
  for (int k = 0; k < 9; ++k) {
    u32x4 o;
#pragma unroll
    for (int e = 0; e < 4; ++e) {
      float a = lo_f(v[k][e]) * inv * lo_f(gv[k][e]);
      float b = hi_f(v[k][e]) * inv * hi_f(gv[k][e]);
      o[e] = pack_bf16(a, b);
    }
    yr[k * 8] = o;
  }
  if (blockIdx.x == 0 && threadIdx.x < 64) {
    asm volatile("s_waitcnt vmcnt(0)" ::: "memory");
    while (__builtin_amdgcn_s_memrealtime() - t0 < 4000ull) { __builtin_amdgcn_s_sleep(1); }
  }
}

extern "C" void kernel_launch(void* const* d_in, const int* in_sizes, int n_in, void* d_out, int out_size,
                              void* d_ws, size_t ws_size, hipStream_t stream) {
  const u32x4* x = (const u32x4*)d_in[0];
  const u32x4* g = (const u32x4*)d_in[1];
  u32x4* y = (u32x4*)d_out;
  int rows = in_sizes[0] / D;
  int blocks = (rows * 8 + 255) / 256;
  rmsnorm_k<<<blocks, 256, 0, stream>>>(x, g, y, rows);
}
```

```cpp
#include <hip/hip_runtime.h>
#include <stdint.h>

static constexpr int D = 576;
static constexpr int ROWB = D * 2;
static constexpr int BLOCK = 256;
static constexpr int ROWS_PER_BLOCK = BLOCK / 8;

__global__ __launch_bounds__(BLOCK) void rmsnorm_k(const void* x, const void* g, void* y, unsigned nbytes,
                                                   unsigned xcd_stride) {
  __shared__ __attribute__((aligned(16))) char lds[(BLOCK / 64) * 2 * ROWB];
  const unsigned ldsa = (unsigned)(uintptr_t)(__attribute__((address_space(3))) char*)lds;
  asm volatile(
      "s_load_dwordx2 s[4:5], %[ka], 0x0\n"
      "s_load_dwordx2 s[8:9], %[ka], 0x8\n"
      "s_load_dwordx2 s[12:13], %[ka], 0x10\n"
      "s_load_dword s6, %[ka], 0x18\n"
      "s_load_dword s14, %[ka], 0x18\n"
      "s_load_dword s20, %[ka], 0x1c\n"
      "s_mov_b32 s7, 0x20000\n"
      "s_mov_b32 s10, 1152\n"
      "s_mov_b32 s11, 0x20000\n"
      "s_mov_b32 s15, 0x20000\n"
      "v_lshrrev_b32 v1, 3, %[tid]\n"
      "v_and_b32 v110, 7, %[tid]\n"
      "s_lshr_b32 s18, %[bid], 3\n"
      "s_and_b32 s19, %[bid], 7\n"
      "s_mul_i32 s18, s18, 36864\n"
      "v_mul_u32_u24 v1, 0x480, v1\n"
      "v_lshlrev_b32 v110, 4, v110\n"
      "v_add3_u32 v1, v1, v110, s18\n"
      "v_and_b32 v116, 63, %[tid]\n"
      "v_lshrrev_b32 v111, 6, %[tid]\n"
      "v_lshlrev_b32 v116, 4, v116\n"
      "v_mul_u32_u24 v111, 2304, v111\n"
      "v_add3_u32 v117, v110, v111, %[lds]\n"
      "v_add3_u32 v111, v116, v111, %[lds]\n"
      "v_mov_b32 v112, 0\n"
      "v_mov_b32 v113, 0\n"
      "v_mov_b32 v114, 0x358637bd\n"
      "s_mov_b32 s16, 0xffff\n"
      "s_mov_b32 s17, 0xffff0000\n"
      "s_waitcnt lgkmcnt(0)\n"
      "s_mul_i32 s19, s19, s20\n"
      "v_add_u32 v1, s19, v1\n"
      "buffer_load_dwordx4 v[38:41], v116, s[8:11], 0 offen\n"
      "s_mov_b64 exec, 0xff\n"
      "buffer_load_dwordx4 v[42:45], v116, s[8:11], 0 offen offset:1024\n"
      "s_mov_b64 exec, -1\n"
      "buffer_load_dwordx4 v[2:5], v1, s[4:7], 0 offen nt\n"
      "buffer_load_dwordx4 v[6:9], v1, s[4:7], 0 offen offset:128 nt\n"
      "buffer_load_dwordx4 v[10:13], v1, s[4:7], 0 offen offset:256 nt\n"
      "buffer_load_dwordx4 v[14:17], v1, s[4:7], 0 offen offset:384 nt\n"
      "buffer_load_dwordx4 v[18:21], v1, s[4:7], 0 offen offset:512 nt\n"
      "buffer_load_dwordx4 v[22:25], v1, s[4:7], 0 offen offset:640 nt\n"
      "buffer_load_dwordx4 v[26:29], v1, s[4:7], 0 offen offset:768 nt\n"
      "buffer_load_dwordx4 v[30:33], v1, s[4:7], 0 offen offset:896 nt\n"
      "buffer_load_dwordx4 v[34:37], v1, s[4:7], 0 offen offset:1024 nt\n"
      "s_waitcnt vmcnt(10)\n"
      "v_and_b32 v46, s16, v38\n"
      "v_and_b32 v47, s17, v38\n"
      "v_and_b32 v48, s16, v39\n"
      "v_and_b32 v49, s17, v39\n"
      "v_and_b32 v50, s16, v40\n"
      "v_and_b32 v51, s17, v40\n"
      "v_and_b32 v52, s16, v41\n"
      "v_and_b32 v53, s17, v41\n"
      "ds_write_b128 v111, v[46:49]\n"
      "ds_write_b128 v111, v[50:53] offset:1152\n"
      "s_waitcnt vmcnt(9)\n"
      "v_and_b32 v54, s16, v42\n"
      "v_and_b32 v55, s17, v42\n"
      "v_and_b32 v56, s16, v43\n"
      "v_and_b32 v57, s17, v43\n"
      "v_and_b32 v58, s16, v44\n"
      "v_and_b32 v59, s17, v44\n"
      "v_and_b32 v60, s16, v45\n"
      "v_and_b32 v61, s17, v45\n"
      "s_mov_b64 exec, 0xff\n"
      "ds_write_b128 v111, v[54:57] offset:1024\n"
      "ds_write_b128 v111, v[58:61] offset:2176\n"
      "s_mov_b64 exec, -1\n"
      "s_waitcnt lgkmcnt(0)\n"
      "ds_read_b128 v[38:41], v117\n"
      "ds_read_b128 v[42:45], v117 offset:1152\n"
      "ds_read_b128 v[46:49], v117 offset:128\n"
      "ds_read_b128 v[50:53], v117 offset:1280\n"
      "ds_read_b128 v[54:57], v117 offset:256\n"
      "ds_read_b128 v[58:61], v117 offset:1408\n"
      "ds_read_b128 v[62:65], v117 offset:384\n"
      "ds_read_b128 v[66:69], v117 offset:1536\n"
      "ds_read_b128 v[70:73], v117 offset:512\n"
      "ds_read_b128 v[74:77], v117 offset:1664\n"
      "ds_read_b128 v[78:81], v117 offset:640\n"
      "ds_read_b128 v[82:85], v117 offset:1792\n"
      "ds_read_b128 v[86:89], v117 offset:768\n"
      "ds_read_b128 v[90:93], v117 offset:1920\n"
      "s_waitcnt vmcnt(8)\n"
      "v_dot2c_f32_bf16 v112, v2, v2\n"
      "v_dot2c_f32_bf16 v113, v3, v3\n"
      "v_dot2c_f32_bf16 v112, v4, v4\n"
      "v_dot2c_f32_bf16 v113, v5, v5\n"
      "s_waitcnt vmcnt(7)\n"
      "v_dot2c_f32_bf16 v112, v6, v6\n"
      "v_dot2c_f32_bf16 v113, v7, v7\n"
      "v_dot2c_f32_bf16 v112, v8, v8\n"
      "v_dot2c_f32_bf16 v113, v9, v9\n"
      "s_waitcnt vmcnt(6)\n"
      "v_dot2c_f32_bf16 v112, v10, v10\n"
      "v_dot2c_f32_bf16 v113, v11, v11\n"
      "v_dot2c_f32_bf16 v112, v12, v12\n"
      "v_dot2c_f32_bf16 v113, v13, v13\n"
      "s_waitcnt vmcnt(5)\n"
      "v_dot2c_f32_bf16 v112, v14, v14\n"
      "v_dot2c_f32_bf16 v113, v15, v15\n"
      "v_dot2c_f32_bf16 v112, v16, v16\n"
      "v_dot2c_f32_bf16 v113, v17, v17\n"
      "s_waitcnt vmcnt(4)\n"
      "v_dot2c_f32_bf16 v112, v18, v18\n"
      "v_dot2c_f32_bf16 v113, v19, v19\n"
      "v_dot2c_f32_bf16 v112, v20, v20\n"
      "v_dot2c_f32_bf16 v113, v21, v21\n"
      "s_waitcnt vmcnt(3)\n"
      "v_dot2c_f32_bf16 v112, v22, v22\n"
      "v_dot2c_f32_bf16 v113, v23, v23\n"
      "v_dot2c_f32_bf16 v112, v24, v24\n"
      "v_dot2c_f32_bf16 v113, v25, v25\n"
      "s_waitcnt vmcnt(2)\n"
      "v_dot2c_f32_bf16 v112, v26, v26\n"
      "v_dot2c_f32_bf16 v113, v27, v27\n"
      "v_dot2c_f32_bf16 v112, v28, v28\n"
      "v_dot2c_f32_bf16 v113, v29, v29\n"
      "s_waitcnt vmcnt(1)\n"
      "v_dot2c_f32_bf16 v112, v30, v30\n"
      "v_dot2c_f32_bf16 v113, v31, v31\n"
      "v_dot2c_f32_bf16 v112, v32, v32\n"
      "v_dot2c_f32_bf16 v113, v33, v33\n"
      "s_waitcnt vmcnt(0)\n"
      "v_dot2c_f32_bf16 v112, v34, v34\n"
      "v_dot2c_f32_bf16 v113, v35, v35\n"
      "v_dot2c_f32_bf16 v112, v36, v36\n"
      "v_dot2c_f32_bf16 v113, v37, v37\n"
      "s_nop 0\n"
      "s_nop 0\n"
      "s_nop 0\n"
      "v_add_f32 v112, v112, v113\n"
      "s_nop 0\n"
      "s_nop 0\n"
      "v_add_f32_dpp v112, v112, v112 quad_perm:[1,0,3,2] row_mask:0xf bank_mask:0xf\n"
      "s_nop 0\n"
      "s_nop 0\n"
      "v_add_f32_dpp v112, v112, v112 quad_perm:[2,3,0,1] row_mask:0xf bank_mask:0xf\n"
      "s_nop 0\n"
      "s_nop 0\n"
      "v_add_f32_dpp v112, v112, v112 row_half_mirror row_mask:0xf bank_mask:0xf\n"
      "v_fmac_f32 v114, 0x3ae38e39, v112\n"
      "v_rsq_f32 v114, v114\n"
      "s_nop 0\n"
      "s_waitcnt lgkmcnt(12)\n"
      "ds_read_b128 v[94:97], v117 offset:896\n"
      "ds_read_b128 v[98:101], v117 offset:2048\n"
      "v_dot2_f32_bf16 v38, v2, v38, 0\n"
      "v_dot2_f32_bf16 v39, v2, v39, 0\n"
      "v_dot2_f32_bf16 v40, v3, v40, 0\n"
      "v_dot2_f32_bf16 v41, v3, v41, 0\n"
      "v_dot2_f32_bf16 v42, v4, v42, 0\n"
      "v_dot2_f32_bf16 v43, v4, v43, 0\n"
      "v_dot2_f32_bf16 v44, v5, v44, 0\n"
      "v_dot2_f32_bf16 v45, v5, v45, 0\n"
      "v_pk_mul_f32 v[38:39], v[38:39], v[114:115] op_sel_hi:[1,0]\n"
      "v_pk_mul_f32 v[40:41], v[40:41], v[114:115] op_sel_hi:[1,0]\n"
      "v_pk_mul_f32 v[42:43], v[42:43], v[114:115] op_sel_hi:[1,0]\n"
      "v_pk_mul_f32 v[44:45], v[44:45], v[114:115] op_sel_hi:[1,0]\n"
      "v_cvt_pk_bf16_f32 v2, v38, v39\n"
      "v_cvt_pk_bf16_f32 v3, v40, v41\n"
      "v_cvt_pk_bf16_f32 v4, v42, v43\n"
      "v_cvt_pk_bf16_f32 v5, v44, v45\n"
      "s_waitcnt lgkmcnt(12)\n"
      "ds_read_b128 v[102:105], v117 offset:1024\n"
      "ds_read_b128 v[106:109], v117 offset:2176\n"
      "v_dot2_f32_bf16 v46, v6, v46, 0\n"
      "v_dot2_f32_bf16 v47, v6, v47, 0\n"
      "v_dot2_f32_bf16 v48, v7, v48, 0\n"
      "v_dot2_f32_bf16 v49, v7, v49, 0\n"
      "v_dot2_f32_bf16 v50, v8, v50, 0\n"
      "v_dot2_f32_bf16 v51, v8, v51, 0\n"
      "v_dot2_f32_bf16 v52, v9, v52, 0\n"
      "v_dot2_f32_bf16 v53, v9, v53, 0\n"
      "v_pk_mul_f32 v[46:47], v[46:47], v[114:115] op_sel_hi:[1,0]\n"
      "v_pk_mul_f32 v[48:49], v[48:49], v[114:115] op_sel_hi:[1,0]\n"
      "v_pk_mul_f32 v[50:51], v[50:51], v[114:115] op_sel_hi:[1,0]\n"
      "v_pk_mul_f32 v[52:53], v[52:53], v[114:115] op_sel_hi:[1,0]\n"
      "v_cvt_pk_bf16_f32 v6, v46, v47\n"
      "v_cvt_pk_bf16_f32 v7, v48, v49\n"
      "v_cvt_pk_bf16_f32 v8, v50, v51\n"
      "v_cvt_pk_bf16_f32 v9, v52, v53\n"
      "s_waitcnt lgkmcnt(12)\n"
      "v_dot2_f32_bf16 v54, v10, v54, 0\n"
      "v_dot2_f32_bf16 v55, v10, v55, 0\n"
      "v_dot2_f32_bf16 v56, v11, v56, 0\n"
      "v_dot2_f32_bf16 v57, v11, v57, 0\n"
      "v_dot2_f32_bf16 v58, v12, v58, 0\n"
      "v_dot2_f32_bf16 v59, v12, v59, 0\n"
      "v_dot2_f32_bf16 v60, v13, v60, 0\n"
      "v_dot2_f32_bf16 v61, v13, v61, 0\n"
      "v_pk_mul_f32 v[54:55], v[54:55], v[114:115] op_sel_hi:[1,0]\n"
      "v_pk_mul_f32 v[56:57], v[56:57], v[114:115] op_sel_hi:[1,0]\n"
      "v_pk_mul_f32 v[58:59], v[58:59], v[114:115] op_sel_hi:[1,0]\n"
      "v_pk_mul_f32 v[60:61], v[60:61], v[114:115] op_sel_hi:[1,0]\n"
      "v_cvt_pk_bf16_f32 v10, v54, v55\n"
      "v_cvt_pk_bf16_f32 v11, v56, v57\n"
      "v_cvt_pk_bf16_f32 v12, v58, v59\n"
      "v_cvt_pk_bf16_f32 v13, v60, v61\n"
      "s_waitcnt lgkmcnt(10)\n"
      "v_dot2_f32_bf16 v62, v14, v62, 0\n"
      "v_dot2_f32_bf16 v63, v14, v63, 0\n"
      "v_dot2_f32_bf16 v64, v15, v64, 0\n"
      "v_dot2_f32_bf16 v65, v15, v65, 0\n"
      "v_dot2_f32_bf16 v66, v16, v66, 0\n"
      "v_dot2_f32_bf16 v67, v16, v67, 0\n"
      "v_dot2_f32_bf16 v68, v17, v68, 0\n"
      "v_dot2_f32_bf16 v69, v17, v69, 0\n"
      "v_pk_mul_f32 v[62:63], v[62:63], v[114:115] op_sel_hi:[1,0]\n"
      "v_pk_mul_f32 v[64:65], v[64:65], v[114:115] op_sel_hi:[1,0]\n"
      "v_pk_mul_f32 v[66:67], v[66:67], v[114:115] op_sel_hi:[1,0]\n"
      "v_pk_mul_f32 v[68:69], v[68:69], v[114:115] op_sel_hi:[1,0]\n"
      "v_cvt_pk_bf16_f32 v14, v62, v63\n"
      "v_cvt_pk_bf16_f32 v15, v64, v65\n"
      "v_cvt_pk_bf16_f32 v16, v66, v67\n"
      "v_cvt_pk_bf16_f32 v17, v68, v69\n"
      "s_waitcnt lgkmcnt(8)\n"
      "v_dot2_f32_bf16 v70, v18, v70, 0\n"
      "v_dot2_f32_bf16 v71, v18, v71, 0\n"
      "v_dot2_f32_bf16 v72, v19, v72, 0\n"
      "v_dot2_f32_bf16 v73, v19, v73, 0\n"
      "v_dot2_f32_bf16 v74, v20, v74, 0\n"
      "v_dot2_f32_bf16 v75, v20, v75, 0\n"
      "v_dot2_f32_bf16 v76, v21, v76, 0\n"
      "v_dot2_f32_bf16 v77, v21, v77, 0\n"
      "v_pk_mul_f32 v[70:71], v[70:71], v[114:115] op_sel_hi:[1,0]\n"
      "v_pk_mul_f32 v[72:73], v[72:73], v[114:115] op_sel_hi:[1,0]\n"
      "v_pk_mul_f32 v[74:75], v[74:75], v[114:115] op_sel_hi:[1,0]\n"
      "v_pk_mul_f32 v[76:77], v[76:77], v[114:115] op_sel_hi:[1,0]\n"
      "v_cvt_pk_bf16_f32 v18, v70, v71\n"
      "v_cvt_pk_bf16_f32 v19, v72, v73\n"
      "v_cvt_pk_bf16_f32 v20, v74, v75\n"
      "v_cvt_pk_bf16_f32 v21, v76, v77\n"
      "s_waitcnt lgkmcnt(6)\n"
      "v_dot2_f32_bf16 v78, v22, v78, 0\n"
      "v_dot2_f32_bf16 v79, v22, v79, 0\n"
      "v_dot2_f32_bf16 v80, v23, v80, 0\n"
      "v_dot2_f32_bf16 v81, v23, v81, 0\n"
      "v_dot2_f32_bf16 v82, v24, v82, 0\n"
      "v_dot2_f32_bf16 v83, v24, v83, 0\n"
      "v_dot2_f32_bf16 v84, v25, v84, 0\n"
      "v_dot2_f32_bf16 v85, v25, v85, 0\n"
      "v_pk_mul_f32 v[78:79], v[78:79], v[114:115] op_sel_hi:[1,0]\n"
      "v_pk_mul_f32 v[80:81], v[80:81], v[114:115] op_sel_hi:[1,0]\n"
      "v_pk_mul_f32 v[82:83], v[82:83], v[114:115] op_sel_hi:[1,0]\n"
      "v_pk_mul_f32 v[84:85], v[84:85], v[114:115] op_sel_hi:[1,0]\n"
      "v_cvt_pk_bf16_f32 v22, v78, v79\n"
      "v_cvt_pk_bf16_f32 v23, v80, v81\n"
      "v_cvt_pk_bf16_f32 v24, v82, v83\n"
      "v_cvt_pk_bf16_f32 v25, v84, v85\n"
      "s_waitcnt lgkmcnt(4)\n"
      "v_dot2_f32_bf16 v86, v26, v86, 0\n"
      "v_dot2_f32_bf16 v87, v26, v87, 0\n"
      "v_dot2_f32_bf16 v88, v27, v88, 0\n"
      "v_dot2_f32_bf16 v89, v27, v89, 0\n"
      "v_dot2_f32_bf16 v90, v28, v90, 0\n"
      "v_dot2_f32_bf16 v91, v28, v91, 0\n"
      "v_dot2_f32_bf16 v92, v29, v92, 0\n"
      "v_dot2_f32_bf16 v93, v29, v93, 0\n"
      "v_pk_mul_f32 v[86:87], v[86:87], v[114:115] op_sel_hi:[1,0]\n"
      "v_pk_mul_f32 v[88:89], v[88:89], v[114:115] op_sel_hi:[1,0]\n"
      "v_pk_mul_f32 v[90:91], v[90:91], v[114:115] op_sel_hi:[1,0]\n"
      "v_pk_mul_f32 v[92:93], v[92:93], v[114:115] op_sel_hi:[1,0]\n"
      "v_cvt_pk_bf16_f32 v26, v86, v87\n"
      "v_cvt_pk_bf16_f32 v27, v88, v89\n"
      "v_cvt_pk_bf16_f32 v28, v90, v91\n"
      "v_cvt_pk_bf16_f32 v29, v92, v93\n"
      "s_waitcnt lgkmcnt(2)\n"
      "v_dot2_f32_bf16 v94, v30, v94, 0\n"
      "v_dot2_f32_bf16 v95, v30, v95, 0\n"
      "v_dot2_f32_bf16 v96, v31, v96, 0\n"
      "v_dot2_f32_bf16 v97, v31, v97, 0\n"
      "v_dot2_f32_bf16 v98, v32, v98, 0\n"
      "v_dot2_f32_bf16 v99, v32, v99, 0\n"
      "v_dot2_f32_bf16 v100, v33, v100, 0\n"
      "v_dot2_f32_bf16 v101, v33, v101, 0\n"
      "v_pk_mul_f32 v[94:95], v[94:95], v[114:115] op_sel_hi:[1,0]\n"
      "v_pk_mul_f32 v[96:97], v[96:97], v[114:115] op_sel_hi:[1,0]\n"
      "v_pk_mul_f32 v[98:99], v[98:99], v[114:115] op_sel_hi:[1,0]\n"
      "v_pk_mul_f32 v[100:101], v[100:101], v[114:115] op_sel_hi:[1,0]\n"
      "v_cvt_pk_bf16_f32 v30, v94, v95\n"
      "v_cvt_pk_bf16_f32 v31, v96, v97\n"
      "v_cvt_pk_bf16_f32 v32, v98, v99\n"
      "v_cvt_pk_bf16_f32 v33, v100, v101\n"
      "s_waitcnt lgkmcnt(0)\n"
      "v_dot2_f32_bf16 v102, v34, v102, 0\n"
      "v_dot2_f32_bf16 v103, v34, v103, 0\n"
      "v_dot2_f32_bf16 v104, v35, v104, 0\n"
      "v_dot2_f32_bf16 v105, v35, v105, 0\n"
      "v_dot2_f32_bf16 v106, v36, v106, 0\n"
      "v_dot2_f32_bf16 v107, v36, v107, 0\n"
      "v_dot2_f32_bf16 v108, v37, v108, 0\n"
      "v_dot2_f32_bf16 v109, v37, v109, 0\n"
      "v_pk_mul_f32 v[102:103], v[102:103], v[114:115] op_sel_hi:[1,0]\n"
      "v_pk_mul_f32 v[104:105], v[104:105], v[114:115] op_sel_hi:[1,0]\n"
      "v_pk_mul_f32 v[106:107], v[106:107], v[114:115] op_sel_hi:[1,0]\n"
      "v_pk_mul_f32 v[108:109], v[108:109], v[114:115] op_sel_hi:[1,0]\n"
      "v_cvt_pk_bf16_f32 v34, v102, v103\n"
      "v_cvt_pk_bf16_f32 v35, v104, v105\n"
      "v_cvt_pk_bf16_f32 v36, v106, v107\n"
      "v_cvt_pk_bf16_f32 v37, v108, v109\n"
      "s_cmp_ge_u32 %[bid], 256\n"
      "s_cbranch_scc1 .Lrms_nf%=\n"
      "buffer_load_dword v113, v116, s[8:11], 0 offen\n"
      "s_waitcnt vmcnt(0)\n"
      ".Lrms_nf%=:\n"
      "buffer_store_dwordx4 v[2:5], v1, s[12:15], 0 offen sc0 sc1\n"
      "buffer_store_dwordx4 v[6:9], v1, s[12:15], 0 offen offset:128 sc0 sc1\n"
      "buffer_store_dwordx4 v[10:13], v1, s[12:15], 0 offen offset:256 sc0 sc1\n"
      "buffer_store_dwordx4 v[14:17], v1, s[12:15], 0 offen offset:384 sc0 sc1\n"
      "buffer_store_dwordx4 v[18:21], v1, s[12:15], 0 offen offset:512 sc0 sc1\n"
      "buffer_store_dwordx4 v[22:25], v1, s[12:15], 0 offen offset:640 sc0 sc1\n"
      "buffer_store_dwordx4 v[26:29], v1, s[12:15], 0 offen offset:768 sc0 sc1\n"
      "buffer_store_dwordx4 v[30:33], v1, s[12:15], 0 offen offset:896 sc0 sc1\n"
      "buffer_store_dwordx4 v[34:37], v1, s[12:15], 0 offen offset:1024 sc0 sc1\n"
      :
      : [ka] "s"(__builtin_amdgcn_kernarg_segment_ptr()), [bid] "s"(blockIdx.x), [tid] "v"(threadIdx.x), [lds] "s"(ldsa)
      : "memory", "vcc", "scc", "v1", "v2", "v3", "v4", "v5", "v6", "v7", "v8", "v9", "v10", "v11", "v12", "v13",
        "v14", "v15", "v16", "v17", "v18", "v19", "v20", "v21", "v22", "v23", "v24", "v25", "v26", "v27", "v28",
        "v29", "v30", "v31", "v32", "v33", "v34", "v35", "v36", "v37", "v38", "v39", "v40", "v41", "v42", "v43",
        "v44", "v45", "v46", "v47", "v48", "v49", "v50", "v51", "v52", "v53", "v54", "v55", "v56", "v57", "v58",
        "v59", "v60", "v61", "v62", "v63", "v64", "v65", "v66", "v67", "v68", "v69", "v70", "v71", "v72", "v73",
        "v74", "v75", "v76", "v77", "v78", "v79", "v80", "v81", "v82", "v83", "v84", "v85", "v86", "v87", "v88",
        "v89", "v90", "v91", "v92", "v93", "v94", "v95", "v96", "v97", "v98", "v99", "v100", "v101", "v102",
        "v103", "v104", "v105", "v106", "v107", "v108", "v109", "v110", "v111", "v112", "v113", "v114", "v115",
        "v116", "v117", "s4", "s5", "s6", "s7", "s8", "s9", "s10", "s11", "s12", "s13", "s14", "s15", "s16", "s17",
        "s18", "s19", "s20");
}

extern "C" void kernel_launch(void* const* d_in, const int* in_sizes, int n_in, void* d_out, int out_size,
                              void* d_ws, size_t ws_size, hipStream_t stream) {
  const int rows = in_sizes[0] / D;
  const int blocks = ((rows + ROWS_PER_BLOCK - 1) / ROWS_PER_BLOCK + 7) / 8 * 8;
  rmsnorm_k<<<blocks, BLOCK, 0, stream>>>(d_in[0], d_in[1], d_out, (unsigned)rows * ROWB,
                                          (unsigned)(blocks / 8) * ROWS_PER_BLOCK * ROWB);
}
```
